```python
import torch

def _softmax(x, axis=-1):
    return torch.softmax(x, dim=axis)
def _gelu(x):
    return torch.nn.functional.gelu(x, approximate="tanh")
def _silu(x):
    return torch.nn.functional.silu(x)
def _sigmoid(x):
    return torch.sigmoid(x)
def _layernorm(x, axis=-1, eps=1e-5):
    m = x.mean(dim=axis, keepdim=True)
    v = x.var(dim=axis, keepdim=True, unbiased=False)
    return (x - m) / torch.sqrt(v + eps)
def _scatter_add(tgt, idx, src, axis=0):
    return tgt.clone().index_add_(axis, idx, src)
def _segment_sum(src, seg, n):
    out = torch.zeros((n,) + src.shape[1:], dtype=src.dtype)
    return out.index_add_(0, seg, src)
def _pad(x, pw):
    flat = tuple(v for pair in reversed(pw) for v in pair)
    return torch.nn.functional.pad(x, flat)

kernel_name = 'standalone__dense_activation_gelu__prompt'

def setup_inputs():

    _w = (lambda a: (lambda t: t.to(torch.bfloat16) if t.is_floating_point() else t)(torch.as_tensor(a)))
    B, S, D, F = 1, 2048, 512, 1376
    return {
        "x": _w((lambda *s: torch.randn(*s, dtype=torch.bfloat16) * 0.05)(B, S, D)),
        "w1": _w((lambda *s: torch.randn(*s, dtype=torch.bfloat16) * 3.2)(D, F) * (D ** -0.5)),
        "b1": _w((lambda *s: torch.randn(*s, dtype=torch.bfloat16) * 3.2)(F) * 0.01),
        "w2": _w((lambda *s: torch.randn(*s, dtype=torch.bfloat16) * 0.05)(F, D) * (F ** -0.5)),
        "b2": _w((lambda *s: torch.randn(*s, dtype=torch.bfloat16) * 0.1)(D) * 0.01),
    }

def reference(x, w1, b1, w2, b2):
    h = torch.einsum("bsd,df->bsf", x, w1) + b1
    h = _gelu(h)
    return torch.einsum("bsf,fd->bsd", h, w2) + b2
```

```cpp
#include <hip/hip_runtime.h>
#include <hip/hip_bf16.h>

typedef __hip_bfloat16 bf16;
constexpr int S = 2048, D = 512, F = 1376;

__global__ void k1(const bf16* x, const bf16* w1, const bf16* b1, bf16* h) {
    int f = blockIdx.x * 64 + (threadIdx.x & 63);
    int m = blockIdx.y * 4 + (threadIdx.x >> 6);
    if (f >= F) return;
    float acc = 0.f;
    for (int k = 0; k < D; ++k) acc += __bfloat162float(x[m * D + k]) * __bfloat162float(w1[k * F + f]);
    float v = acc + __bfloat162float(b1[f]);
    float u = 0.7978845608028654f * (v + 0.044715f * v * v * v);
    float g = 0.5f * v * (1.f + tanhf(u));
    h[m * F + f] = __float2bfloat16(g);
}
__global__ void k2(const bf16* h, const bf16* w2, const bf16* b2, bf16* out) {
    int n = blockIdx.x * 64 + (threadIdx.x & 63);
    int m = blockIdx.y * 4 + (threadIdx.x >> 6);
    float acc = 0.f;
    for (int k = 0; k < F; ++k) acc += __bfloat162float(h[m * F + k]) * __bfloat162float(w2[k * D + n]);
    out[m * D + n] = __float2bfloat16(acc + __bfloat162float(b2[n]));
}

extern "C" void kernel_launch(void* const* d_in, const int* in_sizes, int n_in,
                              void* d_out, int out_size, void* d_ws, size_t ws_size,
                              hipStream_t stream) {
    const bf16* x = (const bf16*)d_in[0];
    const bf16* w1 = (const bf16*)d_in[1];
    const bf16* b1 = (const bf16*)d_in[2];
    const bf16* w2 = (const bf16*)d_in[3];
    const bf16* b2 = (const bf16*)d_in[4];
    bf16* h = (bf16*)d_ws;
    k1<<<dim3((F + 63) / 64, S / 4), 256, 0, stream>>>(x, w1, b1, h);
    k2<<<dim3(D / 64, S / 4), 256, 0, stream>>>(h, w2, b2, (bf16*)d_out);
}
```

```cpp
#include <hip/hip_runtime.h>
#include <stdint.h>

__global__ __launch_bounds__(512, 2) void mlp_fused(const void* x, const void* w1, const void* b1, const void* w2,
                                                    const void* b2, void* out, void* ws) {
    asm volatile(R"ASM(
    .Lmlp_start:
    s_load_dwordx2 s[8:9], s[0:1], 0x0
    s_load_dwordx2 s[12:13], s[0:1], 0x8
    s_load_dwordx2 s[28:29], s[0:1], 0x10
    s_load_dwordx2 s[16:17], s[0:1], 0x18
    s_load_dwordx2 s[20:21], s[0:1], 0x30
    s_load_dwordx2 s[24:25], s[0:1], 0x28
    s_load_dwordx2 s[30:31], s[0:1], 0x20
    v_readfirstlane_b32 s3, v0
    v_and_b32 v1, 63, v0
    s_lshr_b32 s3, s3, 6
    s_and_b32 s34, s2, 7
    s_lshr_b32 s35, s2, 6
    s_bfe_u32 s36, s2, 0x30003
    s_lshr_b32 s37, s34, 1
    s_lshl_b32 s38, s37, 2
    s_add_u32 s38, s38, s35
    s_and_b32 s39, s34, 1
    s_lshl_b32 s39, s39, 3
    s_add_u32 s39, s39, s36
    s_mul_i32 s44, s39, 3
    s_min_u32 s43, s44, 40
    v_lshrrev_b32 v2, 5, v1
    s_cmp_ge_u32 s3, 4
    s_cbranch_scc1 .Lmlp_compute
    s_mov_b32 s10, 0x200000
    s_mov_b32 s11, 0x20000
    s_mov_b32 s14, 0x158000
    s_mov_b32 s15, 0x20000
    s_lshl_b32 s46, s35, 1
    s_add_u32 s46, s46, s36
    s_add_u32 s46, s46, s37
    s_and_b32 s46, s46, 7
    s_lshl_b32 s47, s38, 17
    s_lshl_b32 s48, s3, 15
    s_add_u32 s47, s47, s48
    s_lshl_b32 s49, s3, 12
    s_mul_i32 s50, s3, 3072
    s_add_u32 s50, s50, 16384
    s_lshl_b32 s51, s43, 6
    v_lshrrev_b32 v8, 3, v1
    v_and_b32 v3, 7, v1
    v_lshrrev_b32 v4, 4, v1
    v_xor_b32 v5, v3, v4
    v_lshlrev_b32 v5, 4, v5
    v_lshl_or_b32 v10, v8, 10, v5
    v_xor_b32 v11, 64, v10
    v_add_u32 v11, 7168, v11
    v_add_u32 v12, 14336, v10
    v_add_u32 v13, 14336, v11
    s_mul_i32 s52, s3, 192
    v_add_u32 v6, s52, v1
    v_lshlrev_b32 v9, 4, v6
    s_movk_i32 s53, 2560
    v_mul_u32_u24 v7, 0xaaab, v6
    v_lshrrev_b32 v7, 19, v7
    v_mad_u32_u24 v14, v7, s53, v9
    v_add_u32 v7, 64, v6
    v_mul_u32_u24 v7, 0xaaab, v7
    v_lshrrev_b32 v7, 19, v7
    v_mad_u32_u24 v15, v7, s53, v9
    v_add_u32 v7, 128, v6
    v_mul_u32_u24 v7, 0xaaab, v7
    v_lshrrev_b32 v7, 19, v7
    v_mad_u32_u24 v16, v7, s53, v9
    s_lshl_b32 s54, s46, 7
    s_add_u32 s54, s54, s47
    s_mul_i32 s55, s46, 176128
    s_add_u32 s55, s55, s51
    s_add_u32 s46, s46, 1
    s_and_b32 s46, s46, 7
    s_lshl_b32 s88, s46, 7
    s_add_u32 s88, s88, s47
    s_mul_i32 s89, s46, 176128
    s_add_u32 s89, s89, s51
    s_add_u32 s46, s46, 1
    s_and_b32 s46, s46, 7
    s_waitcnt lgkmcnt(0)
    s_add_u32 m0, s49, 0
    s_nop 0
    buffer_load_dwordx4 v10, s[8:11], s54 offen offset:0  lds
    buffer_load_dwordx4 v11, s[8:11], s54 offen offset:1024  lds
    buffer_load_dwordx4 v12, s[8:11], s54 offen offset:2048  lds
    buffer_load_dwordx4 v13, s[8:11], s54 offen offset:3072  lds
    s_add_u32 m0, s50, 0
    s_nop 0
    buffer_load_dwordx4 v14, s[12:15], s55 offen offset:0  lds
    buffer_load_dwordx4 v15, s[12:15], s55 offen offset:1024  lds
    buffer_load_dwordx4 v16, s[12:15], s55 offen offset:2048  lds
    s_add_u32 m0, s50, 28672
    s_nop 0
    buffer_load_dwordx4 v14, s[12:15], s89 offen offset:0  lds
    buffer_load_dwordx4 v15, s[12:15], s89 offen offset:1024  lds
    buffer_load_dwordx4 v16, s[12:15], s89 offen offset:2048  lds
    s_add_u32 m0, s49, 28672
    s_nop 0
    buffer_load_dwordx4 v10, s[8:11], s88 offen offset:0  lds
    buffer_load_dwordx4 v11, s[8:11], s88 offen offset:1024  lds
    buffer_load_dwordx4 v12, s[8:11], s88 offen offset:2048  lds
    buffer_load_dwordx4 v13, s[8:11], s88 offen offset:3072  lds
    s_mov_b32 s18, 0x158000
    s_mov_b32 s19, 0x20000
    s_mul_i32 s45, s36, 11
    s_lshr_b32 s45, s45, 3
    s_mul_i32 s59, s35, 3
    s_add_u32 s45, s45, s59
    s_add_u32 s45, s45, s34
    s_sub_u32 s59, s45, 11
    s_cmp_ge_u32 s45, 11
    s_cselect_b32 s45, s59, s45
    s_sub_u32 s59, s45, 11
    s_cmp_ge_u32 s45, 11
    s_cselect_b32 s45, s59, s45
    s_add_u32 s62, s20, 8388608
    s_addc_u32 s63, s21, 0
    v_mov_b32 v23, 0
    global_load_dwordx2 v[24:25], v23, s[62:63] offset:2048 sc1
    v_mov_b32 v30, 159744
    v_mov_b32 v31, 0
    ds_write_b32 v30, v31
    s_lshl_b32 s59, s3, 2
    s_add_u32 s59, s59, 159760
    v_mov_b32 v29, s59
    ds_write_b32 v29, v31
    v_and_b32 v5, 1, v4
    v_lshlrev_b32 v5, 2, v5
    v_xor_b32 v5, v5, v3
    v_lshlrev_b32 v5, 4, v5
    v_lshl_or_b32 v17, v8, 10, v5
    v_add_u32 v18, 7168, v17
    v_add_u32 v19, 14336, v17
    v_add_u32 v20, 21504, v17
    v_lshlrev_b32 v21, 4, v1
    s_lshl_b32 s60, s34, 2
    s_add_u32 s60, s60, s35
    s_lshr_b32 s60, s60, 1
    s_lshl_b32 s60, s60, 7
    s_lshl_b32 s57, s3, 15
    s_lshl_b32 s58, s36, 7
    s_add_u32 s57, s57, s58
    s_waitcnt vmcnt(8)
    s_barrier
    s_lshl_b32 s54, s46, 7
    s_add_u32 s54, s54, s47
    s_mul_i32 s55, s46, 176128
    s_add_u32 s55, s55, s51
    s_add_u32 s46, s46, 1
    s_and_b32 s46, s46, 7
    s_add_u32 m0, s49, 57344
    s_nop 0
    buffer_load_dwordx4 v10, s[8:11], s54 offen offset:0  lds
    buffer_load_dwordx4 v11, s[8:11], s54 offen offset:1024  lds
    buffer_load_dwordx4 v12, s[8:11], s54 offen offset:2048  lds
    buffer_load_dwordx4 v13, s[8:11], s54 offen offset:3072  lds
    s_add_u32 m0, s50, 57344
    s_nop 0
    buffer_load_dwordx4 v14, s[12:15], s55 offen offset:0  lds
    buffer_load_dwordx4 v15, s[12:15], s55 offen offset:1024  lds
    buffer_load_dwordx4 v16, s[12:15], s55 offen offset:2048  lds
    s_lshl_b32 s88, s46, 7
    s_add_u32 s88, s88, s47
    s_mul_i32 s89, s46, 176128
    s_add_u32 s89, s89, s51
    s_add_u32 s46, s46, 1
    s_and_b32 s46, s46, 7
    s_add_u32 m0, s49, 86016
    s_nop 0
    buffer_load_dwordx4 v10, s[8:11], s88 offen offset:0  lds
    buffer_load_dwordx4 v11, s[8:11], s88 offen offset:1024  lds
    buffer_load_dwordx4 v12, s[8:11], s88 offen offset:2048  lds
    buffer_load_dwordx4 v13, s[8:11], s88 offen offset:3072  lds
    s_add_u32 m0, s50, 86016
    s_nop 0
    buffer_load_dwordx4 v14, s[12:15], s89 offen offset:0  lds
    buffer_load_dwordx4 v15, s[12:15], s89 offen offset:1024  lds
    buffer_load_dwordx4 v16, s[12:15], s89 offen offset:2048  lds
    s_waitcnt vmcnt(15)
    s_barrier
    s_lshl_b32 s54, s46, 7
    s_add_u32 s54, s54, s47
    s_mul_i32 s55, s46, 176128
    s_add_u32 s55, s55, s51
    s_add_u32 s46, s46, 1
    s_and_b32 s46, s46, 7
    s_add_u32 m0, s49, 114688
    s_nop 0
    buffer_load_dwordx4 v10, s[8:11], s54 offen offset:0  lds
    buffer_load_dwordx4 v11, s[8:11], s54 offen offset:1024  lds
    buffer_load_dwordx4 v12, s[8:11], s54 offen offset:2048  lds
    buffer_load_dwordx4 v13, s[8:11], s54 offen offset:3072  lds
    s_add_u32 m0, s50, 114688
    s_nop 0
    buffer_load_dwordx4 v14, s[12:15], s55 offen offset:0  lds
    buffer_load_dwordx4 v15, s[12:15], s55 offen offset:1024  lds
    buffer_load_dwordx4 v16, s[12:15], s55 offen offset:2048  lds
    s_waitcnt vmcnt(14)
    s_barrier
    s_lshl_b32 s88, s46, 7
    s_add_u32 s88, s88, s47
    s_mul_i32 s89, s46, 176128
    s_add_u32 s89, s89, s51
    s_add_u32 s46, s46, 1
    s_and_b32 s46, s46, 7
    s_add_u32 m0, s49, 0
    s_nop 0
    buffer_load_dwordx4 v10, s[8:11], s88 offen offset:0  lds
    buffer_load_dwordx4 v11, s[8:11], s88 offen offset:1024  lds
    buffer_load_dwordx4 v12, s[8:11], s88 offen offset:2048  lds
    buffer_load_dwordx4 v13, s[8:11], s88 offen offset:3072  lds
    s_add_u32 m0, s50, 0
    s_nop 0
    buffer_load_dwordx4 v14, s[12:15], s89 offen offset:0  lds
    buffer_load_dwordx4 v15, s[12:15], s89 offen offset:1024  lds
    buffer_load_dwordx4 v16, s[12:15], s89 offen offset:2048  lds
    s_waitcnt vmcnt(14)
    s_barrier
    s_lshl_b32 s54, s46, 7
    s_add_u32 s54, s54, s47
    s_mul_i32 s55, s46, 176128
    s_add_u32 s55, s55, s51
    s_add_u32 s46, s46, 1
    s_and_b32 s46, s46, 7
    s_add_u32 m0, s49, 28672
    s_nop 0
    buffer_load_dwordx4 v10, s[8:11], s54 offen offset:0  lds
    buffer_load_dwordx4 v11, s[8:11], s54 offen offset:1024  lds
    buffer_load_dwordx4 v12, s[8:11], s54 offen offset:2048  lds
    buffer_load_dwordx4 v13, s[8:11], s54 offen offset:3072  lds
    s_add_u32 m0, s50, 28672
    s_nop 0
    buffer_load_dwordx4 v14, s[12:15], s55 offen offset:0  lds
    buffer_load_dwordx4 v15, s[12:15], s55 offen offset:1024  lds
    buffer_load_dwordx4 v16, s[12:15], s55 offen offset:2048  lds
    s_waitcnt vmcnt(14)
    s_barrier
    s_lshl_b32 s88, s46, 7
    s_add_u32 s88, s88, s47
    s_mul_i32 s89, s46, 176128
    s_add_u32 s89, s89, s51
    s_add_u32 s46, s46, 1
    s_and_b32 s46, s46, 7
    s_add_u32 m0, s49, 57344
    s_nop 0
    buffer_load_dwordx4 v10, s[8:11], s88 offen offset:0  lds
    buffer_load_dwordx4 v11, s[8:11], s88 offen offset:1024  lds
    buffer_load_dwordx4 v12, s[8:11], s88 offen offset:2048  lds
    buffer_load_dwordx4 v13, s[8:11], s88 offen offset:3072  lds
    s_add_u32 m0, s50, 57344
    s_nop 0
    buffer_load_dwordx4 v14, s[12:15], s89 offen offset:0  lds
    buffer_load_dwordx4 v15, s[12:15], s89 offen offset:1024  lds
    buffer_load_dwordx4 v16, s[12:15], s89 offen offset:2048  lds
    s_waitcnt vmcnt(14)
    s_barrier
    s_add_u32 s58, s45, 0
    s_sub_u32 s59, s58, 11
    s_cmp_ge_u32 s58, 11
    s_cselect_b32 s58, s59, s58
    s_lshl_b32 s58, s58, 17
    s_add_u32 s58, s58, s57
    s_add_u32 m0, s49, 143360
    s_nop 0
    buffer_load_dwordx4 v17, s[16:19], s58 offen offset:0 lds
    buffer_load_dwordx4 v18, s[16:19], s58 offen offset:1024 lds
    buffer_load_dwordx4 v19, s[16:19], s58 offen offset:2048 lds
    buffer_load_dwordx4 v20, s[16:19], s58 offen offset:3072 lds
    s_waitcnt vmcnt(11)
    s_barrier
    s_add_u32 s58, s45, 1
    s_sub_u32 s59, s58, 11
    s_cmp_ge_u32 s58, 11
    s_cselect_b32 s58, s59, s58
    s_lshl_b32 s58, s58, 17
    s_add_u32 s58, s58, s57
    s_add_u32 m0, s49, 86016
    s_nop 0
    buffer_load_dwordx4 v17, s[16:19], s58 offen offset:0 lds
    buffer_load_dwordx4 v18, s[16:19], s58 offen offset:1024 lds
    buffer_load_dwordx4 v19, s[16:19], s58 offen offset:2048 lds
    buffer_load_dwordx4 v20, s[16:19], s58 offen offset:3072 lds
    s_waitcnt vmcnt(8)
    s_barrier
    v_add_u32 v24, 1, v24
    v_lshl_add_u32 v23, v1, 3, s60
    s_barrier
    s_add_u32 s58, s45, 2
    s_sub_u32 s59, s58, 11
    s_cmp_ge_u32 s58, 11
    s_cselect_b32 s58, s59, s58
    s_lshl_b32 s58, s58, 17
    s_add_u32 s58, s58, s57
    s_add_u32 m0, s49, 102400
    s_nop 0
    s_waitcnt vmcnt(2)
    buffer_load_dwordx4 v17, s[16:19], s58 offen offset:0 lds
    s_waitcnt vmcnt(2)
    buffer_load_dwordx4 v18, s[16:19], s58 offen offset:1024 lds
    s_waitcnt vmcnt(2)
    buffer_load_dwordx4 v19, s[16:19], s58 offen offset:2048 lds
    s_waitcnt vmcnt(2)
    buffer_load_dwordx4 v20, s[16:19], s58 offen offset:3072 lds
    s_add_u32 s58, s45, 3
    s_sub_u32 s59, s58, 11
    s_cmp_ge_u32 s58, 11
    s_cselect_b32 s58, s59, s58
    s_lshl_b32 s58, s58, 17
    s_add_u32 s58, s58, s57
    s_add_u32 m0, s49, 118784
    s_nop 0
    s_waitcnt vmcnt(2)
    buffer_load_dwordx4 v17, s[16:19], s58 offen offset:0 lds
    s_waitcnt vmcnt(2)
    buffer_load_dwordx4 v18, s[16:19], s58 offen offset:1024 lds
    s_waitcnt vmcnt(2)
    buffer_load_dwordx4 v19, s[16:19], s58 offen offset:2048 lds
    s_waitcnt vmcnt(2)
    buffer_load_dwordx4 v20, s[16:19], s58 offen offset:3072 lds
    s_add_u32 s58, s45, 4
    s_sub_u32 s59, s58, 11
    s_cmp_ge_u32 s58, 11
    s_cselect_b32 s58, s59, s58
    s_lshl_b32 s58, s58, 17
    s_add_u32 s58, s58, s57
    s_add_u32 m0, s49, 0
    s_nop 0
    s_waitcnt vmcnt(2)
    buffer_load_dwordx4 v17, s[16:19], s58 offen offset:0 lds
    s_waitcnt vmcnt(2)
    buffer_load_dwordx4 v18, s[16:19], s58 offen offset:1024 lds
    s_waitcnt vmcnt(2)
    buffer_load_dwordx4 v19, s[16:19], s58 offen offset:2048 lds
    s_waitcnt vmcnt(2)
    buffer_load_dwordx4 v20, s[16:19], s58 offen offset:3072 lds
    s_add_u32 s58, s45, 5
    s_sub_u32 s59, s58, 11
    s_cmp_ge_u32 s58, 11
    s_cselect_b32 s58, s59, s58
    s_lshl_b32 s58, s58, 17
    s_add_u32 s58, s58, s57
    s_add_u32 m0, s49, 16384
    s_nop 0
    s_waitcnt vmcnt(2)
    buffer_load_dwordx4 v17, s[16:19], s58 offen offset:0 lds
    s_waitcnt vmcnt(2)
    buffer_load_dwordx4 v18, s[16:19], s58 offen offset:1024 lds
    s_waitcnt vmcnt(2)
    buffer_load_dwordx4 v19, s[16:19], s58 offen offset:2048 lds
    s_waitcnt vmcnt(2)
    buffer_load_dwordx4 v20, s[16:19], s58 offen offset:3072 lds
    s_add_u32 s58, s45, 6
    s_sub_u32 s59, s58, 11
    s_cmp_ge_u32 s58, 11
    s_cselect_b32 s58, s59, s58
    s_lshl_b32 s58, s58, 17
    s_add_u32 s58, s58, s57
    s_add_u32 m0, s49, 32768
    s_nop 0
    s_waitcnt vmcnt(2)
    buffer_load_dwordx4 v17, s[16:19], s58 offen offset:0 lds
    s_waitcnt vmcnt(2)
    buffer_load_dwordx4 v18, s[16:19], s58 offen offset:1024 lds
    s_waitcnt vmcnt(2)
    buffer_load_dwordx4 v19, s[16:19], s58 offen offset:2048 lds
    s_waitcnt vmcnt(2)
    buffer_load_dwordx4 v20, s[16:19], s58 offen offset:3072 lds
    s_add_u32 s58, s45, 7
    s_sub_u32 s59, s58, 11
    s_cmp_ge_u32 s58, 11
    s_cselect_b32 s58, s59, s58
    s_lshl_b32 s58, s58, 17
    s_add_u32 s58, s58, s57
    s_add_u32 m0, s49, 49152
    s_nop 0
    s_waitcnt vmcnt(2)
    buffer_load_dwordx4 v17, s[16:19], s58 offen offset:0 lds
    s_waitcnt vmcnt(2)
    buffer_load_dwordx4 v18, s[16:19], s58 offen offset:1024 lds
    s_waitcnt vmcnt(2)
    buffer_load_dwordx4 v19, s[16:19], s58 offen offset:2048 lds
    s_waitcnt vmcnt(2)
    buffer_load_dwordx4 v20, s[16:19], s58 offen offset:3072 lds
    s_add_u32 s58, s45, 8
    s_sub_u32 s59, s58, 11
    s_cmp_ge_u32 s58, 11
    s_cselect_b32 s58, s59, s58
    s_lshl_b32 s58, s58, 17
    s_add_u32 s58, s58, s57
    s_add_u32 m0, s49, 65536
    s_nop 0
    s_waitcnt vmcnt(2)
    buffer_load_dwordx4 v17, s[16:19], s58 offen offset:0 lds
    s_waitcnt vmcnt(2)
    buffer_load_dwordx4 v18, s[16:19], s58 offen offset:1024 lds
    s_waitcnt vmcnt(2)
    buffer_load_dwordx4 v19, s[16:19], s58 offen offset:2048 lds
    s_waitcnt vmcnt(2)
    buffer_load_dwordx4 v20, s[16:19], s58 offen offset:3072 lds
    s_add_u32 s58, s45, 9
    s_sub_u32 s59, s58, 11
    s_cmp_ge_u32 s58, 11
    s_cselect_b32 s58, s59, s58
    s_lshl_b32 s58, s58, 17
    s_add_u32 s58, s58, s57
    s_waitcnt vmcnt(2)
    buffer_load_dwordx4 v[96:99], v17, s[16:19], s58 offen offset:0
    s_waitcnt vmcnt(2)
    buffer_load_dwordx4 v[100:103], v18, s[16:19], s58 offen offset:1024
    s_waitcnt vmcnt(2)
    buffer_load_dwordx4 v[104:107], v19, s[16:19], s58 offen offset:2048
    s_waitcnt vmcnt(2)
    buffer_load_dwordx4 v[108:111], v20, s[16:19], s58 offen offset:3072
    s_add_u32 s58, s45, 10
    s_sub_u32 s59, s58, 11
    s_cmp_ge_u32 s58, 11
    s_cselect_b32 s58, s59, s58
    s_lshl_b32 s58, s58, 17
    s_add_u32 s58, s58, s57
    s_waitcnt vmcnt(2)
    buffer_load_dwordx4 v[112:115], v17, s[16:19], s58 offen offset:0
    s_waitcnt vmcnt(2)
    buffer_load_dwordx4 v[116:119], v18, s[16:19], s58 offen offset:1024
    s_waitcnt vmcnt(2)
    buffer_load_dwordx4 v[120:123], v19, s[16:19], s58 offen offset:2048
    s_waitcnt vmcnt(2)
    buffer_load_dwordx4 v[124:127], v20, s[16:19], s58 offen offset:3072
    s_cmp_lg_u32 s3, 0
    s_cbranch_scc1 .Lmlp_nopoll
    s_mov_b64 exec, 0xffff
    s_mov_b32 s61, 0
    .Lmlp_poll:
    global_load_dwordx2 v[26:27], v23, s[62:63] sc1
    s_waitcnt vmcnt(0)
    v_cmp_eq_u64 vcc, v[26:27], v[24:25]
    s_cmp_eq_u64 vcc, exec
    s_cbranch_scc1 .Lmlp_poll_done
    s_add_u32 s61, s61, 1
    s_cmp_lt_u32 s61, 0x10000
    s_sleep 1
    s_cbranch_scc1 .Lmlp_poll
    .Lmlp_poll_done:
    s_mov_b64 exec, -1
    .Lmlp_nopoll:
    s_waitcnt vmcnt(0) lgkmcnt(0)
    s_barrier
    s_barrier
    s_add_u32 s58, s49, 143360
    v_add_u32 v22, s58, v21
    ds_write_b128 v22, v[96:99] offset:0
    ds_write_b128 v22, v[100:103] offset:1024
    ds_write_b128 v22, v[104:107] offset:2048
    ds_write_b128 v22, v[108:111] offset:3072
    s_add_u32 s58, s49, 86016
    v_add_u32 v22, s58, v21
    ds_write_b128 v22, v[112:115] offset:0
    ds_write_b128 v22, v[116:119] offset:1024
    ds_write_b128 v22, v[120:123] offset:2048
    ds_write_b128 v22, v[124:127] offset:3072
    s_waitcnt lgkmcnt(0)
    s_barrier
    s_barrier
    s_or_b32 s61, s2, s3
    s_cmp_lg_u32 s61, 0
    s_cbranch_scc1 .Lmlp_ld_done
    v_mov_b32 v2, 0
    v_lshlrev_b32 v3, 5, v1
    s_mov_b32 s61, 0
    .Lmlp_ep_loop:
    global_load_dwordx4 v[8:11], v3, s[62:63] sc1
    global_load_dwordx4 v[12:15], v3, s[62:63] offset:16 sc1
    s_waitcnt vmcnt(0)
    v_cmp_eq_u64 vcc, v[8:9], v[24:25]
    v_cmp_eq_u64 s[56:57], v[10:11], v[24:25]
    s_and_b64 vcc, vcc, s[56:57]
    v_cmp_eq_u64 s[56:57], v[12:13], v[24:25]
    s_and_b64 vcc, vcc, s[56:57]
    v_cmp_eq_u64 s[56:57], v[14:15], v[24:25]
    s_and_b64 vcc, vcc, s[56:57]
    s_cmp_eq_u64 vcc, -1
    s_cbranch_scc1 .Lmlp_ep_done
    s_add_u32 s61, s61, 1
    s_cmp_lt_u32 s61, 0x10000
    s_sleep 2
    s_cbranch_scc1 .Lmlp_ep_loop
    .Lmlp_ep_done:
    s_mov_b64 exec, 1
    global_store_dwordx2 v2, v[24:25], s[62:63] offset:2048 sc1
    s_mov_b64 exec, -1
    .Lmlp_ld_done:
    s_endpgm
    .p2align 6
    .Lmlp_compute:
    s_sub_u32 s4, s3, 4
    s_setprio 1
    s_and_b32 s5, s4, 1
    s_lshr_b32 s6, s4, 1
    v_lshlrev_b32 v3, 3, v2
    v_mov_b32 v4, 0
    s_lshl_b32 s46, s43, 6
    s_lshl_b32 s47, s36, 7
    s_lshl_b32 s50, s5, 6
    s_add_u32 s47, s47, s50
    s_waitcnt lgkmcnt(0)
    s_add_u32 s28, s28, s46
    s_addc_u32 s29, s29, 0
    global_load_dwordx2 v[128:129], v3, s[28:29] offset:0
    global_load_dwordx2 v[130:131], v3, s[28:29] offset:16
    global_load_dwordx2 v[132:133], v3, s[28:29] offset:32
    global_load_dwordx2 v[134:135], v3, s[28:29] offset:48
    global_load_dwordx2 v[136:137], v3, s[28:29] offset:64
    global_load_dwordx2 v[138:139], v3, s[28:29] offset:80
    global_load_dwordx2 v[140:141], v3, s[28:29] offset:96
    global_load_dwordx2 v[142:143], v3, s[28:29] offset:112
    global_load_dwordx2 v[144:145], v3, s[28:29] offset:128
    global_load_dwordx2 v[146:147], v3, s[28:29] offset:144
    global_load_dwordx2 v[148:149], v3, s[28:29] offset:160
    global_load_dwordx2 v[150:151], v3, s[28:29] offset:176
    s_mul_i32 s45, s36, 11
    s_lshr_b32 s45, s45, 3
    s_mul_i32 s59, s35, 3
    s_add_u32 s45, s45, s59
    s_add_u32 s45, s45, s34
    s_sub_u32 s59, s45, 11
    s_cmp_ge_u32 s45, 11
    s_cselect_b32 s45, s59, s45
    s_sub_u32 s59, s45, 11
    s_cmp_ge_u32 s45, 11
    s_cselect_b32 s45, s59, s45
    s_add_u32 s32, s20, 8388608
    s_addc_u32 s33, s21, 0
    s_add_u32 s30, s30, s47
    s_addc_u32 s31, s31, 0
    s_mov_b32 s26, 0x200000
    s_mov_b32 s27, 0x20000
    s_mov_b32 s22, 0x560000
    s_mov_b32 s23, 0x20000
    s_lshl_b32 s51, s34, 2
    s_add_u32 s51, s51, s35
    s_lshl_b32 s52, s51, 1
    s_add_u32 s52, s52, s6
    s_lshl_b32 s53, s38, 2
    s_add_u32 s53, s53, s4
    s_mul_i32 s54, s53, 88064
    s_mul_i32 s55, s52, 88064
    s_lshl_b32 s56, s52, 15
    s_add_u32 s56, s56, s47
    v_and_b32 v76, 31, v1
    v_lshlrev_b32 v76, 10, v76
    v_lshl_add_u32 v76, v2, 4, v76
    v_and_b32 v5, 31, v1
    s_lshl_b32 s59, s4, 5
    v_add_u32 v5, s59, v5
    v_bfe_u32 v6, v5, 1, 3
    v_lshlrev_b32 v7, 7, v5
    v_or_b32 v8, 0, v2
    v_xor_b32 v8, v8, v6
    v_lshl_add_u32 v192, v8, 4, v7
    v_or_b32 v8, 2, v2
    v_xor_b32 v8, v8, v6
    v_lshl_add_u32 v193, v8, 4, v7
    v_or_b32 v8, 4, v2
    v_xor_b32 v8, v8, v6
    v_lshl_add_u32 v194, v8, 4, v7
    v_or_b32 v8, 6, v2
    v_xor_b32 v8, v8, v6
    v_lshl_add_u32 v195, v8, 4, v7
    v_bfe_u32 v8, v1, 4, 1
    v_bfe_u32 v9, v1, 2, 2
    v_and_b32 v10, 3, v1
    v_lshl_add_u32 v11, v2, 3, v9
    v_mul_u32_u24 v11, 192, v11
    v_lshlrev_b32 v12, 5, v8
    v_lshl_add_u32 v12, v10, 3, v12
    v_add_u32 v11, v11, v12
    v_add_u32 v196, 16384, v11
    v_bfe_u32 v8, v1, 4, 1
    v_bfe_u32 v9, v1, 2, 2
    v_and_b32 v10, 3, v1
    v_lshl_add_u32 v11, v2, 2, v9
    s_lshl_b32 s57, s5, 4
    v_add_u32 v11, s57, v11
    v_lshlrev_b32 v11, 7, v11
    v_and_b32 v12, 1, v10
    v_lshl_add_u32 v11, v12, 3, v11
    v_lshrrev_b32 v12, 1, v10
    v_lshl_add_u32 v12, v8, 1, v12
    v_lshrrev_b32 v13, 1, v9
    v_lshlrev_b32 v13, 2, v13
    v_xor_b32 v12, v12, v13
    v_lshl_add_u32 v56, v12, 4, v11
    s_lshl_b32 s57, s5, 6
    v_xor_b32 v56, s57, v56
    v_xor_b32 v57, 64, v56
    v_add_u32 v58, 65536, v56
    v_add_u32 v59, 65536, v57
    v_add_u32 v60, 86016, v56
    v_add_u32 v61, 86016, v57
    v_add_u32 v62, 143360, v56
    v_add_u32 v63, 143360, v57
    v_lshlrev_b32 v64, 4, v1
    v_add_u32 v65, 0x1000, v64
    v_mov_b32 v176, 0
    v_mov_b32 v177, 0
    v_mov_b32 v178, 0
    v_mov_b32 v179, 0
    v_mov_b32 v180, 0
    v_mov_b32 v181, 0
    v_mov_b32 v182, 0
    v_mov_b32 v183, 0
    v_mov_b32 v184, 0
    v_mov_b32 v185, 0
    v_mov_b32 v186, 0
    v_mov_b32 v187, 0
    v_mov_b32 v188, 0
    v_mov_b32 v189, 0
    v_mov_b32 v190, 0
    v_mov_b32 v191, 0
    s_mov_b32 s7, 0
    s_waitcnt vmcnt(0)
    global_load_dwordx2 v[74:75], v4, s[32:33] offset:2048 sc1
    global_load_dwordx2 v[66:67], v3, s[30:31] offset:0
    global_load_dwordx2 v[68:69], v3, s[30:31] offset:16
    global_load_dwordx2 v[70:71], v3, s[30:31] offset:32
    global_load_dwordx2 v[72:73], v3, s[30:31] offset:48
    v_lshlrev_b32 v80, 16, v128
    v_and_b32 v81, 0xffff0000, v128
    v_lshlrev_b32 v82, 16, v129
    v_and_b32 v83, 0xffff0000, v129
    v_lshlrev_b32 v84, 16, v130
    v_and_b32 v85, 0xffff0000, v130
    v_lshlrev_b32 v86, 16, v131
    v_and_b32 v87, 0xffff0000, v131
    v_lshlrev_b32 v88, 16, v132
    v_and_b32 v89, 0xffff0000, v132
    v_lshlrev_b32 v90, 16, v133
    v_and_b32 v91, 0xffff0000, v133
    v_lshlrev_b32 v92, 16, v134
    v_and_b32 v93, 0xffff0000, v134
    v_lshlrev_b32 v94, 16, v135
    v_and_b32 v95, 0xffff0000, v135
    v_lshlrev_b32 v96, 16, v136
    v_and_b32 v97, 0xffff0000, v136
    v_lshlrev_b32 v98, 16, v137
    v_and_b32 v99, 0xffff0000, v137
    v_lshlrev_b32 v100, 16, v138
    v_and_b32 v101, 0xffff0000, v138
    v_lshlrev_b32 v102, 16, v139
    v_and_b32 v103, 0xffff0000, v139
    v_lshlrev_b32 v104, 16, v140
    v_and_b32 v105, 0xffff0000, v140
    v_lshlrev_b32 v106, 16, v141
    v_and_b32 v107, 0xffff0000, v141
    v_lshlrev_b32 v108, 16, v142
    v_and_b32 v109, 0xffff0000, v142
    v_lshlrev_b32 v110, 16, v143
    v_and_b32 v111, 0xffff0000, v143
    v_lshlrev_b32 v112, 16, v144
    v_and_b32 v113, 0xffff0000, v144
    v_lshlrev_b32 v114, 16, v145
    v_and_b32 v115, 0xffff0000, v145
    v_lshlrev_b32 v116, 16, v146
    v_and_b32 v117, 0xffff0000, v146
    v_lshlrev_b32 v118, 16, v147
    v_and_b32 v119, 0xffff0000, v147
    v_lshlrev_b32 v120, 16, v148
    v_and_b32 v121, 0xffff0000, v148
    v_lshlrev_b32 v122, 16, v149
    v_and_b32 v123, 0xffff0000, v149
    v_lshlrev_b32 v124, 16, v150
    v_and_b32 v125, 0xffff0000, v150
    v_lshlrev_b32 v126, 16, v151
    v_and_b32 v127, 0xffff0000, v151
    s_mov_b32 s57, 0
    s_mov_b32 s58, 8
    v_mov_b32 v197, v192
    v_mov_b32 v198, v193
    v_mov_b32 v199, v194
    v_mov_b32 v200, v195
    v_mov_b32 v201, v196
    .Lmlp_p1:
    s_barrier
    ds_read_b128 v[128:131], v197
    ds_read_b64_tr_b16 v[132:133], v201 offset:0
    ds_read_b64_tr_b16 v[134:135], v201 offset:768
    ds_read_b64_tr_b16 v[136:137], v201 offset:64
    ds_read_b64_tr_b16 v[138:139], v201 offset:832
    ds_read_b64_tr_b16 v[140:141], v201 offset:128
    ds_read_b64_tr_b16 v[142:143], v201 offset:896
    v_mfma_f32_32x32x16_bf16 v[80:95], v[180:183], v[176:179], v[80:95]
    ds_read_b128 v[144:147], v198
    ds_read_b64_tr_b16 v[148:149], v201 offset:3072
    ds_read_b64_tr_b16 v[150:151], v201 offset:3840
    ds_read_b64_tr_b16 v[152:153], v201 offset:3136
    v_mfma_f32_32x32x16_bf16 v[96:111], v[184:187], v[176:179], v[96:111]
    ds_read_b64_tr_b16 v[154:155], v201 offset:3904
    ds_read_b64_tr_b16 v[156:157], v201 offset:3200
    ds_read_b64_tr_b16 v[158:159], v201 offset:3968
    v_mfma_f32_32x32x16_bf16 v[112:127], v[188:191], v[176:179], v[112:127]
    s_waitcnt lgkmcnt(7)
    v_mfma_f32_32x32x16_bf16 v[80:95], v[132:135], v[128:131], v[80:95]
    ds_read_b128 v[160:163], v199
    ds_read_b64_tr_b16 v[164:165], v201 offset:6144
    ds_read_b64_tr_b16 v[166:167], v201 offset:6912
    ds_read_b64_tr_b16 v[168:169], v201 offset:6208
    v_mfma_f32_32x32x16_bf16 v[96:111], v[136:139], v[128:131], v[96:111]
    ds_read_b64_tr_b16 v[170:171], v201 offset:6976
    ds_read_b64_tr_b16 v[172:173], v201 offset:6272
    ds_read_b64_tr_b16 v[174:175], v201 offset:7040
    v_mfma_f32_32x32x16_bf16 v[112:127], v[140:143], v[128:131], v[112:127]
    s_waitcnt lgkmcnt(7)
    v_mfma_f32_32x32x16_bf16 v[80:95], v[148:151], v[144:147], v[80:95]
    ds_read_b128 v[176:179], v200
    ds_read_b64_tr_b16 v[180:181], v201 offset:9216
    ds_read_b64_tr_b16 v[182:183], v201 offset:9984
    ds_read_b64_tr_b16 v[184:185], v201 offset:9280
    v_mfma_f32_32x32x16_bf16 v[96:111], v[152:155], v[144:147], v[96:111]
    ds_read_b64_tr_b16 v[186:187], v201 offset:10048
    ds_read_b64_tr_b16 v[188:189], v201 offset:9344
    ds_read_b64_tr_b16 v[190:191], v201 offset:10112
    v_mfma_f32_32x32x16_bf16 v[112:127], v[156:159], v[144:147], v[112:127]
    s_add_u32 s57, s57, 28672
    s_cmp_eq_u32 s57, 143360
    s_cselect_b32 s57, 0, s57
    s_waitcnt lgkmcnt(7)
    v_mfma_f32_32x32x16_bf16 v[80:95], v[164:167], v[160:163], v[80:95]
    s_sub_u32 s58, s58, 1
    v_mfma_f32_32x32x16_bf16 v[96:111], v[168:171], v[160:163], v[96:111]
    v_mfma_f32_32x32x16_bf16 v[112:127], v[172:175], v[160:163], v[112:127]
    s_cmp_lg_u32 s58, 0
    s_waitcnt lgkmcnt(0)
    v_add_u32 v197, s57, v192
    v_add_u32 v198, s57, v193
    v_add_u32 v199, s57, v194
    v_add_u32 v200, s57, v195
    v_add_u32 v201, s57, v196
    s_cbranch_scc1 .Lmlp_p1
    s_barrier
    v_mfma_f32_32x32x16_bf16 v[80:95], v[180:183], v[176:179], v[80:95]
    v_mfma_f32_32x32x16_bf16 v[96:111], v[184:187], v[176:179], v[96:111]
    v_mfma_f32_32x32x16_bf16 v[112:127], v[188:191], v[176:179], v[112:127]
    s_waitcnt vmcnt(0)
    v_add_u32 v74, 1, v74
    v_mov_b32 v10, 0xc0135761
    v_mov_b32 v11, 0xc0135761
    v_mov_b32 v12, 0xbdd2d3e8
    v_mov_b32 v13, 0xbdd2d3e8
    s_lshl_b32 s59, s43, 11
    s_add_u32 s59, s59, s54
    s_nop 7
    .Lmlp_gelu:
    s_add_u32 s60, s43, 0
    s_cmp_lt_u32 s60, s44
    s_cbranch_scc1 .Lmlp_skip0
    v_pk_mul_f32 v[160:161], v[80:81], v[80:81]
    v_pk_mul_f32 v[162:163], v[82:83], v[82:83]
    v_pk_mul_f32 v[164:165], v[84:85], v[84:85]
    v_pk_mul_f32 v[166:167], v[86:87], v[86:87]
    v_pk_fma_f32 v[160:161], v[160:161], v[12:13], v[10:11]
    v_pk_fma_f32 v[162:163], v[162:163], v[12:13], v[10:11]
    v_pk_fma_f32 v[164:165], v[164:165], v[12:13], v[10:11]
    v_pk_fma_f32 v[166:167], v[166:167], v[12:13], v[10:11]
    v_pk_mul_f32 v[160:161], v[80:81], v[160:161]
    v_pk_mul_f32 v[162:163], v[82:83], v[162:163]
    v_pk_mul_f32 v[164:165], v[84:85], v[164:165]
    v_pk_mul_f32 v[166:167], v[86:87], v[166:167]
    v_exp_f32 v160, v160
    v_exp_f32 v161, v161
    v_exp_f32 v162, v162
    v_exp_f32 v163, v163
    v_exp_f32 v164, v164
    v_exp_f32 v165, v165
    v_exp_f32 v166, v166
    v_exp_f32 v167, v167
    v_pk_add_f32 v[160:161], v[160:161], 1.0 op_sel_hi:[1,0]
    v_pk_add_f32 v[162:163], v[162:163], 1.0 op_sel_hi:[1,0]
    v_pk_add_f32 v[164:165], v[164:165], 1.0 op_sel_hi:[1,0]
    v_pk_add_f32 v[166:167], v[166:167], 1.0 op_sel_hi:[1,0]
    v_rcp_f32 v160, v160
    v_rcp_f32 v161, v161
    v_rcp_f32 v162, v162
    v_rcp_f32 v163, v163
    v_rcp_f32 v164, v164
    v_rcp_f32 v165, v165
    v_rcp_f32 v166, v166
    v_rcp_f32 v167, v167
    v_pk_mul_f32 v[160:161], v[80:81], v[160:161]
    v_pk_mul_f32 v[162:163], v[82:83], v[162:163]
    v_pk_mul_f32 v[164:165], v[84:85], v[164:165]
    v_pk_mul_f32 v[166:167], v[86:87], v[166:167]
    v_cvt_pk_bf16_f32 v128, v160, v161
    v_cvt_pk_bf16_f32 v129, v162, v163
    v_cvt_pk_bf16_f32 v130, v164, v165
    v_cvt_pk_bf16_f32 v131, v166, v167
    buffer_store_dwordx4 v[128:131], v64, s[20:23], s59 offen offset:0 sc1
    v_pk_mul_f32 v[168:169], v[88:89], v[88:89]
    v_pk_mul_f32 v[170:171], v[90:91], v[90:91]
    v_pk_mul_f32 v[172:173], v[92:93], v[92:93]
    v_pk_mul_f32 v[174:175], v[94:95], v[94:95]
    v_pk_fma_f32 v[168:169], v[168:169], v[12:13], v[10:11]
    v_pk_fma_f32 v[170:171], v[170:171], v[12:13], v[10:11]
    v_pk_fma_f32 v[172:173], v[172:173], v[12:13], v[10:11]
    v_pk_fma_f32 v[174:175], v[174:175], v[12:13], v[10:11]
    v_pk_mul_f32 v[168:169], v[88:89], v[168:169]
    v_pk_mul_f32 v[170:171], v[90:91], v[170:171]
    v_pk_mul_f32 v[172:173], v[92:93], v[172:173]
    v_pk_mul_f32 v[174:175], v[94:95], v[174:175]
    v_exp_f32 v168, v168
    v_exp_f32 v169, v169
    v_exp_f32 v170, v170
    v_exp_f32 v171, v171
    v_exp_f32 v172, v172
    v_exp_f32 v173, v173
    v_exp_f32 v174, v174
    v_exp_f32 v175, v175
    v_pk_add_f32 v[168:169], v[168:169], 1.0 op_sel_hi:[1,0]
    v_pk_add_f32 v[170:171], v[170:171], 1.0 op_sel_hi:[1,0]
    v_pk_add_f32 v[172:173], v[172:173], 1.0 op_sel_hi:[1,0]
    v_pk_add_f32 v[174:175], v[174:175], 1.0 op_sel_hi:[1,0]
    v_rcp_f32 v168, v168
    v_rcp_f32 v169, v169
    v_rcp_f32 v170, v170
    v_rcp_f32 v171, v171
    v_rcp_f32 v172, v172
    v_rcp_f32 v173, v173
    v_rcp_f32 v174, v174
    v_rcp_f32 v175, v175
    v_pk_mul_f32 v[168:169], v[88:89], v[168:169]
    v_pk_mul_f32 v[170:171], v[90:91], v[170:171]
    v_pk_mul_f32 v[172:173], v[92:93], v[172:173]
    v_pk_mul_f32 v[174:175], v[94:95], v[174:175]
    v_cvt_pk_bf16_f32 v132, v168, v169
    v_cvt_pk_bf16_f32 v133, v170, v171
    v_cvt_pk_bf16_f32 v134, v172, v173
    v_cvt_pk_bf16_f32 v135, v174, v175
    buffer_store_dwordx4 v[132:135], v64, s[20:23], s59 offen offset:1024 sc1
    .Lmlp_skip0:
    s_add_u32 s60, s43, 1
    s_cmp_lt_u32 s60, s44
    s_cbranch_scc1 .Lmlp_skip1
    v_pk_mul_f32 v[160:161], v[96:97], v[96:97]
    v_pk_mul_f32 v[162:163], v[98:99], v[98:99]
    v_pk_mul_f32 v[164:165], v[100:101], v[100:101]
    v_pk_mul_f32 v[166:167], v[102:103], v[102:103]
    v_pk_fma_f32 v[160:161], v[160:161], v[12:13], v[10:11]
    v_pk_fma_f32 v[162:163], v[162:163], v[12:13], v[10:11]
    v_pk_fma_f32 v[164:165], v[164:165], v[12:13], v[10:11]
    v_pk_fma_f32 v[166:167], v[166:167], v[12:13], v[10:11]
    v_pk_mul_f32 v[160:161], v[96:97], v[160:161]
    v_pk_mul_f32 v[162:163], v[98:99], v[162:163]
    v_pk_mul_f32 v[164:165], v[100:101], v[164:165]
    v_pk_mul_f32 v[166:167], v[102:103], v[166:167]
    v_exp_f32 v160, v160
    v_exp_f32 v161, v161
    v_exp_f32 v162, v162
    v_exp_f32 v163, v163
    v_exp_f32 v164, v164
    v_exp_f32 v165, v165
    v_exp_f32 v166, v166
    v_exp_f32 v167, v167
    v_pk_add_f32 v[160:161], v[160:161], 1.0 op_sel_hi:[1,0]
    v_pk_add_f32 v[162:163], v[162:163], 1.0 op_sel_hi:[1,0]
    v_pk_add_f32 v[164:165], v[164:165], 1.0 op_sel_hi:[1,0]
    v_pk_add_f32 v[166:167], v[166:167], 1.0 op_sel_hi:[1,0]
    v_rcp_f32 v160, v160
    v_rcp_f32 v161, v161
    v_rcp_f32 v162, v162
    v_rcp_f32 v163, v163
    v_rcp_f32 v164, v164
    v_rcp_f32 v165, v165
    v_rcp_f32 v166, v166
    v_rcp_f32 v167, v167
    v_pk_mul_f32 v[160:161], v[96:97], v[160:161]
    v_pk_mul_f32 v[162:163], v[98:99], v[162:163]
    v_pk_mul_f32 v[164:165], v[100:101], v[164:165]
    v_pk_mul_f32 v[166:167], v[102:103], v[166:167]
    v_cvt_pk_bf16_f32 v136, v160, v161
    v_cvt_pk_bf16_f32 v137, v162, v163
    v_cvt_pk_bf16_f32 v138, v164, v165
    v_cvt_pk_bf16_f32 v139, v166, v167
    buffer_store_dwordx4 v[136:139], v64, s[20:23], s59 offen offset:2048 sc1
    v_pk_mul_f32 v[168:169], v[104:105], v[104:105]
    v_pk_mul_f32 v[170:171], v[106:107], v[106:107]
    v_pk_mul_f32 v[172:173], v[108:109], v[108:109]
    v_pk_mul_f32 v[174:175], v[110:111], v[110:111]
    v_pk_fma_f32 v[168:169], v[168:169], v[12:13], v[10:11]
    v_pk_fma_f32 v[170:171], v[170:171], v[12:13], v[10:11]
    v_pk_fma_f32 v[172:173], v[172:173], v[12:13], v[10:11]
    v_pk_fma_f32 v[174:175], v[174:175], v[12:13], v[10:11]
    v_pk_mul_f32 v[168:169], v[104:105], v[168:169]
    v_pk_mul_f32 v[170:171], v[106:107], v[170:171]
    v_pk_mul_f32 v[172:173], v[108:109], v[172:173]
    v_pk_mul_f32 v[174:175], v[110:111], v[174:175]
    v_exp_f32 v168, v168
    v_exp_f32 v169, v169
    v_exp_f32 v170, v170
    v_exp_f32 v171, v171
    v_exp_f32 v172, v172
    v_exp_f32 v173, v173
    v_exp_f32 v174, v174
    v_exp_f32 v175, v175
    v_pk_add_f32 v[168:169], v[168:169], 1.0 op_sel_hi:[1,0]
    v_pk_add_f32 v[170:171], v[170:171], 1.0 op_sel_hi:[1,0]
    v_pk_add_f32 v[172:173], v[172:173], 1.0 op_sel_hi:[1,0]
    v_pk_add_f32 v[174:175], v[174:175], 1.0 op_sel_hi:[1,0]
    v_rcp_f32 v168, v168
    v_rcp_f32 v169, v169
    v_rcp_f32 v170, v170
    v_rcp_f32 v171, v171
    v_rcp_f32 v172, v172
    v_rcp_f32 v173, v173
    v_rcp_f32 v174, v174
    v_rcp_f32 v175, v175
    v_pk_mul_f32 v[168:169], v[104:105], v[168:169]
    v_pk_mul_f32 v[170:171], v[106:107], v[170:171]
    v_pk_mul_f32 v[172:173], v[108:109], v[172:173]
    v_pk_mul_f32 v[174:175], v[110:111], v[174:175]
    v_cvt_pk_bf16_f32 v140, v168, v169
    v_cvt_pk_bf16_f32 v141, v170, v171
    v_cvt_pk_bf16_f32 v142, v172, v173
    v_cvt_pk_bf16_f32 v143, v174, v175
    buffer_store_dwordx4 v[140:143], v64, s[20:23], s59 offen offset:3072 sc1
    .Lmlp_skip1:
    s_add_u32 s60, s43, 2
    s_cmp_lt_u32 s60, s44
    s_cbranch_scc1 .Lmlp_skip2
    v_pk_mul_f32 v[160:161], v[112:113], v[112:113]
    v_pk_mul_f32 v[162:163], v[114:115], v[114:115]
    v_pk_mul_f32 v[164:165], v[116:117], v[116:117]
    v_pk_mul_f32 v[166:167], v[118:119], v[118:119]
    v_pk_fma_f32 v[160:161], v[160:161], v[12:13], v[10:11]
    v_pk_fma_f32 v[162:163], v[162:163], v[12:13], v[10:11]
    v_pk_fma_f32 v[164:165], v[164:165], v[12:13], v[10:11]
    v_pk_fma_f32 v[166:167], v[166:167], v[12:13], v[10:11]
    v_pk_mul_f32 v[160:161], v[112:113], v[160:161]
    v_pk_mul_f32 v[162:163], v[114:115], v[162:163]
    v_pk_mul_f32 v[164:165], v[116:117], v[164:165]
    v_pk_mul_f32 v[166:167], v[118:119], v[166:167]
    v_exp_f32 v160, v160
    v_exp_f32 v161, v161
    v_exp_f32 v162, v162
    v_exp_f32 v163, v163
    v_exp_f32 v164, v164
    v_exp_f32 v165, v165
    v_exp_f32 v166, v166
    v_exp_f32 v167, v167
    v_pk_add_f32 v[160:161], v[160:161], 1.0 op_sel_hi:[1,0]
    v_pk_add_f32 v[162:163], v[162:163], 1.0 op_sel_hi:[1,0]
    v_pk_add_f32 v[164:165], v[164:165], 1.0 op_sel_hi:[1,0]
    v_pk_add_f32 v[166:167], v[166:167], 1.0 op_sel_hi:[1,0]
    v_rcp_f32 v160, v160
    v_rcp_f32 v161, v161
    v_rcp_f32 v162, v162
    v_rcp_f32 v163, v163
    v_rcp_f32 v164, v164
    v_rcp_f32 v165, v165
    v_rcp_f32 v166, v166
    v_rcp_f32 v167, v167
    v_pk_mul_f32 v[160:161], v[112:113], v[160:161]
    v_pk_mul_f32 v[162:163], v[114:115], v[162:163]
    v_pk_mul_f32 v[164:165], v[116:117], v[164:165]
    v_pk_mul_f32 v[166:167], v[118:119], v[166:167]
    v_cvt_pk_bf16_f32 v144, v160, v161
    v_cvt_pk_bf16_f32 v145, v162, v163
    v_cvt_pk_bf16_f32 v146, v164, v165
    v_cvt_pk_bf16_f32 v147, v166, v167
    buffer_store_dwordx4 v[144:147], v65, s[20:23], s59 offen offset:0 sc1
    v_pk_mul_f32 v[168:169], v[120:121], v[120:121]
    v_pk_mul_f32 v[170:171], v[122:123], v[122:123]
    v_pk_mul_f32 v[172:173], v[124:125], v[124:125]
    v_pk_mul_f32 v[174:175], v[126:127], v[126:127]
    v_pk_fma_f32 v[168:169], v[168:169], v[12:13], v[10:11]
    v_pk_fma_f32 v[170:171], v[170:171], v[12:13], v[10:11]
    v_pk_fma_f32 v[172:173], v[172:173], v[12:13], v[10:11]
    v_pk_fma_f32 v[174:175], v[174:175], v[12:13], v[10:11]
    v_pk_mul_f32 v[168:169], v[120:121], v[168:169]
    v_pk_mul_f32 v[170:171], v[122:123], v[170:171]
    v_pk_mul_f32 v[172:173], v[124:125], v[172:173]
    v_pk_mul_f32 v[174:175], v[126:127], v[174:175]
    v_exp_f32 v168, v168
    v_exp_f32 v169, v169
    v_exp_f32 v170, v170
    v_exp_f32 v171, v171
    v_exp_f32 v172, v172
    v_exp_f32 v173, v173
    v_exp_f32 v174, v174
    v_exp_f32 v175, v175
    v_pk_add_f32 v[168:169], v[168:169], 1.0 op_sel_hi:[1,0]
    v_pk_add_f32 v[170:171], v[170:171], 1.0 op_sel_hi:[1,0]
    v_pk_add_f32 v[172:173], v[172:173], 1.0 op_sel_hi:[1,0]
    v_pk_add_f32 v[174:175], v[174:175], 1.0 op_sel_hi:[1,0]
    v_rcp_f32 v168, v168
    v_rcp_f32 v169, v169
    v_rcp_f32 v170, v170
    v_rcp_f32 v171, v171
    v_rcp_f32 v172, v172
    v_rcp_f32 v173, v173
    v_rcp_f32 v174, v174
    v_rcp_f32 v175, v175
    v_pk_mul_f32 v[168:169], v[120:121], v[168:169]
    v_pk_mul_f32 v[170:171], v[122:123], v[170:171]
    v_pk_mul_f32 v[172:173], v[124:125], v[172:173]
    v_pk_mul_f32 v[174:175], v[126:127], v[174:175]
    v_cvt_pk_bf16_f32 v148, v168, v169
    v_cvt_pk_bf16_f32 v149, v170, v171
    v_cvt_pk_bf16_f32 v150, v172, v173
    v_cvt_pk_bf16_f32 v151, v174, v175
    buffer_store_dwordx4 v[148:151], v65, s[20:23], s59 offen offset:1024 sc1
    .Lmlp_skip2:
    s_waitcnt vmcnt(0)
    s_lshl_b32 s60, s38, 7
    s_lshl_b32 s61, s39, 3
    s_add_u32 s60, s60, s61
    s_mov_b64 exec, 1
    v_mov_b32 v4, 159744
    v_mov_b32 v5, 1
    ds_add_rtn_u32 v5, v4, v5
    v_mov_b32 v4, s60
    s_waitcnt lgkmcnt(0)
    v_cmp_eq_u32 vcc, 3, v5
    s_cbranch_vccz .Lmlp_nopub
    global_store_dwordx2 v4, v[74:75], s[32:33] sc1
    .Lmlp_nopub:
    s_mov_b64 exec, -1
    s_add_u32 s20, s20, s55
    s_addc_u32 s21, s21, 0
    s_mov_b32 s22, 88064
    s_lshl_b32 s60, s5, 10
    s_mov_b32 s61, s45
    s_lshl_b32 s8, s61, 13
    s_add_u32 s8, s8, s60
    s_add_u32 s61, s61, 1
    s_cmp_eq_u32 s61, 11
    s_cselect_b32 s61, 0, s61
    s_lshl_b32 s9, s61, 13
    s_add_u32 s9, s9, s60
    s_add_u32 s61, s61, 1
    s_cmp_eq_u32 s61, 11
    s_cselect_b32 s61, 0, s61
    s_lshl_b32 s10, s61, 13
    s_add_u32 s10, s10, s60
    s_add_u32 s61, s61, 1
    s_cmp_eq_u32 s61, 11
    s_cselect_b32 s61, 0, s61
    s_lshl_b32 s11, s61, 13
    s_add_u32 s11, s11, s60
    s_add_u32 s61, s61, 1
    s_cmp_eq_u32 s61, 11
    s_cselect_b32 s61, 0, s61
    s_lshl_b32 s12, s61, 13
    s_add_u32 s12, s12, s60
    s_add_u32 s61, s61, 1
    s_cmp_eq_u32 s61, 11
    s_cselect_b32 s61, 0, s61
    s_lshl_b32 s13, s61, 13
    s_add_u32 s13, s13, s60
    s_add_u32 s61, s61, 1
    s_cmp_eq_u32 s61, 11
    s_cselect_b32 s61, 0, s61
    s_lshl_b32 s14, s61, 13
    s_add_u32 s14, s14, s60
    s_add_u32 s61, s61, 1
    s_cmp_eq_u32 s61, 11
    s_cselect_b32 s61, 0, s61
    s_lshl_b32 s15, s61, 13
    s_add_u32 s15, s15, s60
    s_add_u32 s61, s61, 1
    s_cmp_eq_u32 s61, 11
    s_cselect_b32 s61, 0, s61
    s_lshl_b32 s16, s61, 13
    s_add_u32 s16, s16, s60
    s_add_u32 s61, s61, 1
    s_cmp_eq_u32 s61, 11
    s_cselect_b32 s61, 0, s61
    s_lshl_b32 s17, s61, 13
    s_add_u32 s17, s17, s60
    s_add_u32 s61, s61, 1
    s_cmp_eq_u32 s61, 11
    s_cselect_b32 s61, 0, s61
    s_lshl_b32 s18, s61, 13
    s_add_u32 s18, s18, s60
    v_lshlrev_b32 v0, 16, v66
    v_and_b32 v1, 0xffff0000, v66
    v_lshlrev_b32 v2, 16, v67
    v_and_b32 v3, 0xffff0000, v67
    v_lshlrev_b32 v4, 16, v68
    v_and_b32 v5, 0xffff0000, v68
    v_lshlrev_b32 v6, 16, v69
    v_and_b32 v7, 0xffff0000, v69
    v_lshlrev_b32 v8, 16, v70
    v_and_b32 v9, 0xffff0000, v70
    v_lshlrev_b32 v10, 16, v71
    v_and_b32 v11, 0xffff0000, v71
    v_lshlrev_b32 v12, 16, v72
    v_and_b32 v13, 0xffff0000, v72
    v_lshlrev_b32 v14, 16, v73
    v_and_b32 v15, 0xffff0000, v73
    s_lshl_b32 s60, s4, 12
    v_add_u32 v78, s60, v64
    s_xor_b32 s61, s4, 1
    s_lshl_b32 s61, s61, 12
    v_add_u32 v79, s61, v64
    s_barrier
    buffer_load_dwordx4 v[80:83], v64, s[20:23], s8 offen offset:0 sc1
    buffer_load_dwordx4 v[84:87], v64, s[20:23], s8 offen offset:2048 sc1
    buffer_load_dwordx4 v[88:91], v65, s[20:23], s8 offen offset:0 sc1
    buffer_load_dwordx4 v[92:95], v65, s[20:23], s8 offen offset:2048 sc1
    buffer_load_dwordx4 v[96:99], v64, s[20:23], s9 offen offset:0 sc1
    buffer_load_dwordx4 v[100:103], v64, s[20:23], s9 offen offset:2048 sc1
    buffer_load_dwordx4 v[104:107], v65, s[20:23], s9 offen offset:0 sc1
    buffer_load_dwordx4 v[108:111], v65, s[20:23], s9 offen offset:2048 sc1
    buffer_load_dwordx4 v[112:115], v64, s[20:23], s10 offen offset:0 sc1
    buffer_load_dwordx4 v[116:119], v64, s[20:23], s10 offen offset:2048 sc1
    buffer_load_dwordx4 v[120:123], v65, s[20:23], s10 offen offset:0 sc1
    buffer_load_dwordx4 v[124:127], v65, s[20:23], s10 offen offset:2048 sc1
    ds_read_b64_tr_b16 v[32:33], v62 offset:0
    ds_read_b64_tr_b16 v[34:35], v62 offset:1024
    ds_read_b64_tr_b16 v[36:37], v63 offset:0
    ds_read_b64_tr_b16 v[38:39], v63 offset:1024
    ds_read_b64_tr_b16 v[40:41], v62 offset:4096
    ds_read_b64_tr_b16 v[42:43], v62 offset:5120
    ds_read_b64_tr_b16 v[44:45], v63 offset:4096
    ds_read_b64_tr_b16 v[46:47], v63 offset:5120
    s_waitcnt vmcnt(11) lgkmcnt(4)
    v_mfma_f32_32x32x16_bf16 v[16:31], v[36:39], v[80:83], 0
    ds_read_b64_tr_b16 v[48:49], v62 offset:8192
    ds_read_b64_tr_b16 v[50:51], v62 offset:9216
    ds_read_b64_tr_b16 v[52:53], v63 offset:8192
    ds_read_b64_tr_b16 v[54:55], v63 offset:9216
    buffer_load_dwordx4 v[128:131], v64, s[20:23], s11 offen offset:0 sc1
    v_mfma_f32_32x32x16_bf16 v[0:15], v[32:35], v[80:83], v[0:15]
    s_waitcnt vmcnt(11) lgkmcnt(4)
    v_mfma_f32_32x32x16_bf16 v[16:31], v[44:47], v[84:87], v[16:31]
    ds_read_b64_tr_b16 v[32:33], v62 offset:12288
    ds_read_b64_tr_b16 v[34:35], v62 offset:13312
    ds_read_b64_tr_b16 v[36:37], v63 offset:12288
    ds_read_b64_tr_b16 v[38:39], v63 offset:13312
    buffer_load_dwordx4 v[132:135], v64, s[20:23], s11 offen offset:2048 sc1
    v_mfma_f32_32x32x16_bf16 v[0:15], v[40:43], v[84:87], v[0:15]
    s_waitcnt vmcnt(11) lgkmcnt(4)
    v_mfma_f32_32x32x16_bf16 v[16:31], v[52:55], v[88:91], v[16:31]
    ds_read_b64_tr_b16 v[40:41], v60 offset:0
    ds_read_b64_tr_b16 v[42:43], v60 offset:1024
    ds_read_b64_tr_b16 v[44:45], v61 offset:0
    ds_read_b64_tr_b16 v[46:47], v61 offset:1024
    buffer_load_dwordx4 v[136:139], v65, s[20:23], s11 offen offset:0 sc1
    v_mfma_f32_32x32x16_bf16 v[0:15], v[48:51], v[88:91], v[0:15]
    s_waitcnt vmcnt(11) lgkmcnt(4)
    v_mfma_f32_32x32x16_bf16 v[16:31], v[36:39], v[92:95], v[16:31]
    ds_read_b64_tr_b16 v[48:49], v60 offset:4096
    ds_read_b64_tr_b16 v[50:51], v60 offset:5120
    ds_read_b64_tr_b16 v[52:53], v61 offset:4096
    ds_read_b64_tr_b16 v[54:55], v61 offset:5120
    buffer_load_dwordx4 v[140:143], v65, s[20:23], s11 offen offset:2048 sc1
    v_mfma_f32_32x32x16_bf16 v[0:15], v[32:35], v[92:95], v[0:15]
    s_waitcnt vmcnt(11) lgkmcnt(4)
    v_mfma_f32_32x32x16_bf16 v[16:31], v[44:47], v[96:99], v[16:31]
    ds_read_b64_tr_b16 v[32:33], v60 offset:8192
    ds_read_b64_tr_b16 v[34:35], v60 offset:9216
    ds_read_b64_tr_b16 v[36:37], v61 offset:8192
    ds_read_b64_tr_b16 v[38:39], v61 offset:9216
    buffer_load_dwordx4 v[144:147], v64, s[20:23], s12 offen offset:0 sc1
    v_mfma_f32_32x32x16_bf16 v[0:15], v[40:43], v[96:99], v[0:15]
    s_waitcnt vmcnt(11) lgkmcnt(4)
    v_mfma_f32_32x32x16_bf16 v[16:31], v[52:55], v[100:103], v[16:31]
    ds_read_b64_tr_b16 v[40:41], v60 offset:12288
    ds_read_b64_tr_b16 v[42:43], v60 offset:13312
    ds_read_b64_tr_b16 v[44:45], v61 offset:12288
    ds_read_b64_tr_b16 v[46:47], v61 offset:13312
    buffer_load_dwordx4 v[148:151], v64, s[20:23], s12 offen offset:2048 sc1
    v_mfma_f32_32x32x16_bf16 v[0:15], v[48:51], v[100:103], v[0:15]
    s_waitcnt vmcnt(11) lgkmcnt(4)
    v_mfma_f32_32x32x16_bf16 v[16:31], v[36:39], v[104:107], v[16:31]
    ds_read_b64_tr_b16 v[48:49], v60 offset:16384
    ds_read_b64_tr_b16 v[50:51], v60 offset:17408
    ds_read_b64_tr_b16 v[52:53], v61 offset:16384
    ds_read_b64_tr_b16 v[54:55], v61 offset:17408
    buffer_load_dwordx4 v[152:155], v65, s[20:23], s12 offen offset:0 sc1
    v_mfma_f32_32x32x16_bf16 v[0:15], v[32:35], v[104:107], v[0:15]
    s_waitcnt vmcnt(11) lgkmcnt(4)
    v_mfma_f32_32x32x16_bf16 v[16:31], v[44:47], v[108:111], v[16:31]
    ds_read_b64_tr_b16 v[32:33], v60 offset:20480
    ds_read_b64_tr_b16 v[34:35], v60 offset:21504
    ds_read_b64_tr_b16 v[36:37], v61 offset:20480
    ds_read_b64_tr_b16 v[38:39], v61 offset:21504
    buffer_load_dwordx4 v[156:159], v65, s[20:23], s12 offen offset:2048 sc1
    v_mfma_f32_32x32x16_bf16 v[0:15], v[40:43], v[108:111], v[0:15]
    s_waitcnt vmcnt(11) lgkmcnt(4)
    v_mfma_f32_32x32x16_bf16 v[16:31], v[52:55], v[112:115], v[16:31]
    ds_read_b64_tr_b16 v[40:41], v60 offset:24576
    ds_read_b64_tr_b16 v[42:43], v60 offset:25600
    ds_read_b64_tr_b16 v[44:45], v61 offset:24576
    ds_read_b64_tr_b16 v[46:47], v61 offset:25600
    buffer_load_dwordx4 v[160:163], v64, s[20:23], s13 offen offset:0 sc1
    v_mfma_f32_32x32x16_bf16 v[0:15], v[48:51], v[112:115], v[0:15]
    s_waitcnt vmcnt(11) lgkmcnt(4)
    v_mfma_f32_32x32x16_bf16 v[16:31], v[36:39], v[116:119], v[16:31]
    ds_read_b64_tr_b16 v[48:49], v60 offset:28672
    ds_read_b64_tr_b16 v[50:51], v60 offset:29696
    ds_read_b64_tr_b16 v[52:53], v61 offset:28672
    ds_read_b64_tr_b16 v[54:55], v61 offset:29696
    buffer_load_dwordx4 v[164:167], v64, s[20:23], s13 offen offset:2048 sc1
    v_mfma_f32_32x32x16_bf16 v[0:15], v[32:35], v[116:119], v[0:15]
    s_waitcnt vmcnt(11) lgkmcnt(4)
    v_mfma_f32_32x32x16_bf16 v[16:31], v[44:47], v[120:123], v[16:31]
    ds_read_b64_tr_b16 v[32:33], v60 offset:32768
    ds_read_b64_tr_b16 v[34:35], v60 offset:33792
    ds_read_b64_tr_b16 v[36:37], v61 offset:32768
    ds_read_b64_tr_b16 v[38:39], v61 offset:33792
    buffer_load_dwordx4 v[168:171], v65, s[20:23], s13 offen offset:0 sc1
    v_mfma_f32_32x32x16_bf16 v[0:15], v[40:43], v[120:123], v[0:15]
    s_waitcnt vmcnt(11) lgkmcnt(4)
    v_mfma_f32_32x32x16_bf16 v[16:31], v[52:55], v[124:127], v[16:31]
    ds_read_b64_tr_b16 v[40:41], v60 offset:36864
    ds_read_b64_tr_b16 v[42:43], v60 offset:37888
    ds_read_b64_tr_b16 v[44:45], v61 offset:36864
    ds_read_b64_tr_b16 v[46:47], v61 offset:37888
    buffer_load_dwordx4 v[172:175], v65, s[20:23], s13 offen offset:2048 sc1
    v_mfma_f32_32x32x16_bf16 v[0:15], v[48:51], v[124:127], v[0:15]
    s_waitcnt vmcnt(11) lgkmcnt(4)
    v_mfma_f32_32x32x16_bf16 v[16:31], v[36:39], v[128:131], v[16:31]
    ds_read_b64_tr_b16 v[48:49], v60 offset:40960
    ds_read_b64_tr_b16 v[50:51], v60 offset:41984
    ds_read_b64_tr_b16 v[52:53], v61 offset:40960
    ds_read_b64_tr_b16 v[54:55], v61 offset:41984
    buffer_load_dwordx4 v[176:179], v64, s[20:23], s14 offen offset:0 sc1
    v_mfma_f32_32x32x16_bf16 v[0:15], v[32:35], v[128:131], v[0:15]
    s_waitcnt vmcnt(11) lgkmcnt(4)
    v_mfma_f32_32x32x16_bf16 v[16:31], v[44:47], v[132:135], v[16:31]
    ds_read_b64_tr_b16 v[32:33], v60 offset:45056
    ds_read_b64_tr_b16 v[34:35], v60 offset:46080
    ds_read_b64_tr_b16 v[36:37], v61 offset:45056
    ds_read_b64_tr_b16 v[38:39], v61 offset:46080
    buffer_load_dwordx4 v[180:183], v64, s[20:23], s14 offen offset:2048 sc1
    v_mfma_f32_32x32x16_bf16 v[0:15], v[40:43], v[132:135], v[0:15]
    s_waitcnt vmcnt(11) lgkmcnt(4)
    v_mfma_f32_32x32x16_bf16 v[16:31], v[52:55], v[136:139], v[16:31]
    ds_read_b64_tr_b16 v[40:41], v56 offset:0
    ds_read_b64_tr_b16 v[42:43], v56 offset:1024
    ds_read_b64_tr_b16 v[44:45], v57 offset:0
    ds_read_b64_tr_b16 v[46:47], v57 offset:1024
    buffer_load_dwordx4 v[184:187], v65, s[20:23], s14 offen offset:0 sc1
    v_mfma_f32_32x32x16_bf16 v[0:15], v[48:51], v[136:139], v[0:15]
    s_waitcnt vmcnt(11) lgkmcnt(4)
    v_mfma_f32_32x32x16_bf16 v[16:31], v[36:39], v[140:143], v[16:31]
    ds_read_b64_tr_b16 v[48:49], v56 offset:4096
    ds_read_b64_tr_b16 v[50:51], v56 offset:5120
    ds_read_b64_tr_b16 v[52:53], v57 offset:4096
    ds_read_b64_tr_b16 v[54:55], v57 offset:5120
    buffer_load_dwordx4 v[188:191], v65, s[20:23], s14 offen offset:2048 sc1
    v_mfma_f32_32x32x16_bf16 v[0:15], v[32:35], v[140:143], v[0:15]
    s_waitcnt vmcnt(11) lgkmcnt(4)
    v_mfma_f32_32x32x16_bf16 v[16:31], v[44:47], v[144:147], v[16:31]
    ds_read_b64_tr_b16 v[32:33], v56 offset:8192
    ds_read_b64_tr_b16 v[34:35], v56 offset:9216
    ds_read_b64_tr_b16 v[36:37], v57 offset:8192
    ds_read_b64_tr_b16 v[38:39], v57 offset:9216
    buffer_load_dwordx4 v[192:195], v64, s[20:23], s15 offen offset:0 sc1
    v_mfma_f32_32x32x16_bf16 v[0:15], v[40:43], v[144:147], v[0:15]
    s_waitcnt vmcnt(11) lgkmcnt(4)
    v_mfma_f32_32x32x16_bf16 v[16:31], v[52:55], v[148:151], v[16:31]
    ds_read_b64_tr_b16 v[40:41], v56 offset:12288
    ds_read_b64_tr_b16 v[42:43], v56 offset:13312
    ds_read_b64_tr_b16 v[44:45], v57 offset:12288
    ds_read_b64_tr_b16 v[46:47], v57 offset:13312
    buffer_load_dwordx4 v[196:199], v64, s[20:23], s15 offen offset:2048 sc1
    v_mfma_f32_32x32x16_bf16 v[0:15], v[48:51], v[148:151], v[0:15]
    s_waitcnt vmcnt(11) lgkmcnt(4)
    v_mfma_f32_32x32x16_bf16 v[16:31], v[36:39], v[152:155], v[16:31]
    s_waitcnt lgkmcnt(0)
    s_barrier
    ds_read_b64_tr_b16 v[48:49], v56 offset:16384
    ds_read_b64_tr_b16 v[50:51], v56 offset:17408
    ds_read_b64_tr_b16 v[52:53], v57 offset:16384
    ds_read_b64_tr_b16 v[54:55], v57 offset:17408
    buffer_load_dwordx4 v[200:203], v65, s[20:23], s15 offen offset:0 sc1
    v_mfma_f32_32x32x16_bf16 v[0:15], v[32:35], v[152:155], v[0:15]
    s_waitcnt vmcnt(11) lgkmcnt(4)
    v_mfma_f32_32x32x16_bf16 v[16:31], v[44:47], v[156:159], v[16:31]
    ds_read_b64_tr_b16 v[32:33], v56 offset:20480
    ds_read_b64_tr_b16 v[34:35], v56 offset:21504
    ds_read_b64_tr_b16 v[36:37], v57 offset:20480
    ds_read_b64_tr_b16 v[38:39], v57 offset:21504
    buffer_load_dwordx4 v[204:207], v65, s[20:23], s15 offen offset:2048 sc1
    v_mfma_f32_32x32x16_bf16 v[0:15], v[40:43], v[156:159], v[0:15]
    s_waitcnt vmcnt(11) lgkmcnt(4)
    v_mfma_f32_32x32x16_bf16 v[16:31], v[52:55], v[160:163], v[16:31]
    ds_read_b64_tr_b16 v[40:41], v56 offset:24576
    ds_read_b64_tr_b16 v[42:43], v56 offset:25600
    ds_read_b64_tr_b16 v[44:45], v57 offset:24576
    ds_read_b64_tr_b16 v[46:47], v57 offset:25600
    buffer_load_dwordx4 v[208:211], v64, s[20:23], s16 offen offset:0 sc1
    v_mfma_f32_32x32x16_bf16 v[0:15], v[48:51], v[160:163], v[0:15]
    s_waitcnt vmcnt(11) lgkmcnt(4)
    v_mfma_f32_32x32x16_bf16 v[16:31], v[36:39], v[164:167], v[16:31]
    ds_read_b64_tr_b16 v[48:49], v56 offset:28672
    ds_read_b64_tr_b16 v[50:51], v56 offset:29696
    ds_read_b64_tr_b16 v[52:53], v57 offset:28672
    ds_read_b64_tr_b16 v[54:55], v57 offset:29696
    buffer_load_dwordx4 v[212:215], v64, s[20:23], s16 offen offset:2048 sc1
    v_mfma_f32_32x32x16_bf16 v[0:15], v[32:35], v[164:167], v[0:15]
    s_waitcnt vmcnt(11) lgkmcnt(4)
    v_mfma_f32_32x32x16_bf16 v[16:31], v[44:47], v[168:171], v[16:31]
    ds_read_b64_tr_b16 v[32:33], v56 offset:32768
    ds_read_b64_tr_b16 v[34:35], v56 offset:33792
    ds_read_b64_tr_b16 v[36:37], v57 offset:32768
    ds_read_b64_tr_b16 v[38:39], v57 offset:33792
    buffer_load_dwordx4 v[216:219], v65, s[20:23], s16 offen offset:0 sc1
    v_mfma_f32_32x32x16_bf16 v[0:15], v[40:43], v[168:171], v[0:15]
    s_waitcnt vmcnt(11) lgkmcnt(4)
    v_mfma_f32_32x32x16_bf16 v[16:31], v[52:55], v[172:175], v[16:31]
    ds_read_b64_tr_b16 v[40:41], v56 offset:36864
    ds_read_b64_tr_b16 v[42:43], v56 offset:37888
    ds_read_b64_tr_b16 v[44:45], v57 offset:36864
    ds_read_b64_tr_b16 v[46:47], v57 offset:37888
    buffer_load_dwordx4 v[220:223], v65, s[20:23], s16 offen offset:2048 sc1
    v_mfma_f32_32x32x16_bf16 v[0:15], v[48:51], v[172:175], v[0:15]
    s_waitcnt vmcnt(11) lgkmcnt(4)
    v_mfma_f32_32x32x16_bf16 v[16:31], v[36:39], v[176:179], v[16:31]
    ds_read_b64_tr_b16 v[48:49], v56 offset:40960
    ds_read_b64_tr_b16 v[50:51], v56 offset:41984
    ds_read_b64_tr_b16 v[52:53], v57 offset:40960
    ds_read_b64_tr_b16 v[54:55], v57 offset:41984
    buffer_load_dwordx4 v[224:227], v64, s[20:23], s17 offen offset:0 sc1
    v_mfma_f32_32x32x16_bf16 v[0:15], v[32:35], v[176:179], v[0:15]
    s_waitcnt vmcnt(11) lgkmcnt(4)
    v_mfma_f32_32x32x16_bf16 v[16:31], v[44:47], v[180:183], v[16:31]
    ds_read_b64_tr_b16 v[32:33], v56 offset:45056
    ds_read_b64_tr_b16 v[34:35], v56 offset:46080
    ds_read_b64_tr_b16 v[36:37], v57 offset:45056
    ds_read_b64_tr_b16 v[38:39], v57 offset:46080
    buffer_load_dwordx4 v[228:231], v64, s[20:23], s17 offen offset:2048 sc1
    v_mfma_f32_32x32x16_bf16 v[0:15], v[40:43], v[180:183], v[0:15]
    s_waitcnt vmcnt(11) lgkmcnt(4)
    v_mfma_f32_32x32x16_bf16 v[16:31], v[52:55], v[184:187], v[16:31]
    ds_read_b64_tr_b16 v[40:41], v56 offset:49152
    ds_read_b64_tr_b16 v[42:43], v56 offset:50176
    ds_read_b64_tr_b16 v[44:45], v57 offset:49152
    ds_read_b64_tr_b16 v[46:47], v57 offset:50176
    buffer_load_dwordx4 v[232:235], v65, s[20:23], s17 offen offset:0 sc1
    v_mfma_f32_32x32x16_bf16 v[0:15], v[48:51], v[184:187], v[0:15]
    s_waitcnt vmcnt(11) lgkmcnt(4)
    v_mfma_f32_32x32x16_bf16 v[16:31], v[36:39], v[188:191], v[16:31]
    ds_read_b64_tr_b16 v[48:49], v56 offset:53248
    ds_read_b64_tr_b16 v[50:51], v56 offset:54272
    ds_read_b64_tr_b16 v[52:53], v57 offset:53248
    ds_read_b64_tr_b16 v[54:55], v57 offset:54272
    buffer_load_dwordx4 v[236:239], v65, s[20:23], s17 offen offset:2048 sc1
    v_mfma_f32_32x32x16_bf16 v[0:15], v[32:35], v[188:191], v[0:15]
    s_waitcnt vmcnt(11) lgkmcnt(4)
    v_mfma_f32_32x32x16_bf16 v[16:31], v[44:47], v[192:195], v[16:31]
    ds_read_b64_tr_b16 v[32:33], v56 offset:57344
    ds_read_b64_tr_b16 v[34:35], v56 offset:58368
    ds_read_b64_tr_b16 v[36:37], v57 offset:57344
    ds_read_b64_tr_b16 v[38:39], v57 offset:58368
    buffer_load_dwordx4 v[240:243], v64, s[20:23], s18 offen offset:0 sc1
    v_mfma_f32_32x32x16_bf16 v[0:15], v[40:43], v[192:195], v[0:15]
    s_waitcnt vmcnt(11) lgkmcnt(4)
    v_mfma_f32_32x32x16_bf16 v[16:31], v[52:55], v[196:199], v[16:31]
    ds_read_b64_tr_b16 v[40:41], v56 offset:61440
    ds_read_b64_tr_b16 v[42:43], v56 offset:62464
    ds_read_b64_tr_b16 v[44:45], v57 offset:61440
    ds_read_b64_tr_b16 v[46:47], v57 offset:62464
    buffer_load_dwordx4 v[244:247], v64, s[20:23], s18 offen offset:2048 sc1
    v_mfma_f32_32x32x16_bf16 v[0:15], v[48:51], v[196:199], v[0:15]
    s_waitcnt vmcnt(11) lgkmcnt(4)
    v_mfma_f32_32x32x16_bf16 v[16:31], v[36:39], v[200:203], v[16:31]
    ds_read_b64_tr_b16 v[48:49], v58 offset:0
    ds_read_b64_tr_b16 v[50:51], v58 offset:1024
    ds_read_b64_tr_b16 v[52:53], v59 offset:0
    ds_read_b64_tr_b16 v[54:55], v59 offset:1024
    buffer_load_dwordx4 v[248:251], v65, s[20:23], s18 offen offset:0 sc1
    v_mfma_f32_32x32x16_bf16 v[0:15], v[32:35], v[200:203], v[0:15]
    s_waitcnt vmcnt(11) lgkmcnt(4)
    v_mfma_f32_32x32x16_bf16 v[16:31], v[44:47], v[204:207], v[16:31]
    ds_read_b64_tr_b16 v[32:33], v58 offset:4096
    ds_read_b64_tr_b16 v[34:35], v58 offset:5120
    ds_read_b64_tr_b16 v[36:37], v59 offset:4096
    ds_read_b64_tr_b16 v[38:39], v59 offset:5120
    buffer_load_dwordx4 v[252:255], v65, s[20:23], s18 offen offset:2048 sc1
    v_mfma_f32_32x32x16_bf16 v[0:15], v[40:43], v[204:207], v[0:15]
    s_waitcnt vmcnt(11) lgkmcnt(4)
    v_mfma_f32_32x32x16_bf16 v[16:31], v[52:55], v[208:211], v[16:31]
    ds_read_b64_tr_b16 v[40:41], v58 offset:8192
    ds_read_b64_tr_b16 v[42:43], v58 offset:9216
    ds_read_b64_tr_b16 v[44:45], v59 offset:8192
    ds_read_b64_tr_b16 v[46:47], v59 offset:9216
    v_mfma_f32_32x32x16_bf16 v[0:15], v[48:51], v[208:211], v[0:15]
    s_waitcnt vmcnt(10) lgkmcnt(4)
    v_mfma_f32_32x32x16_bf16 v[16:31], v[36:39], v[212:215], v[16:31]
    ds_read_b64_tr_b16 v[48:49], v58 offset:12288
    ds_read_b64_tr_b16 v[50:51], v58 offset:13312
    ds_read_b64_tr_b16 v[52:53], v59 offset:12288
    ds_read_b64_tr_b16 v[54:55], v59 offset:13312
    v_mfma_f32_32x32x16_bf16 v[0:15], v[32:35], v[212:215], v[0:15]
    s_waitcnt vmcnt(9) lgkmcnt(4)
    v_mfma_f32_32x32x16_bf16 v[16:31], v[44:47], v[216:219], v[16:31]
    s_waitcnt lgkmcnt(0)
    s_barrier
    ds_read_b64_tr_b16 v[32:33], v62 offset:0
    ds_read_b64_tr_b16 v[34:35], v62 offset:1024
    ds_read_b64_tr_b16 v[36:37], v63 offset:0
    ds_read_b64_tr_b16 v[38:39], v63 offset:1024
    v_mfma_f32_32x32x16_bf16 v[0:15], v[40:43], v[216:219], v[0:15]
    s_waitcnt vmcnt(8) lgkmcnt(4)
    v_mfma_f32_32x32x16_bf16 v[16:31], v[52:55], v[220:223], v[16:31]
    ds_read_b64_tr_b16 v[40:41], v62 offset:4096
    ds_read_b64_tr_b16 v[42:43], v62 offset:5120
    ds_read_b64_tr_b16 v[44:45], v63 offset:4096
    ds_read_b64_tr_b16 v[46:47], v63 offset:5120
    v_mfma_f32_32x32x16_bf16 v[0:15], v[48:51], v[220:223], v[0:15]
    s_waitcnt vmcnt(7) lgkmcnt(4)
    v_mfma_f32_32x32x16_bf16 v[16:31], v[36:39], v[224:227], v[16:31]
    ds_read_b64_tr_b16 v[48:49], v62 offset:8192
    ds_read_b64_tr_b16 v[50:51], v62 offset:9216
    ds_read_b64_tr_b16 v[52:53], v63 offset:8192
    ds_read_b64_tr_b16 v[54:55], v63 offset:9216
    v_mfma_f32_32x32x16_bf16 v[0:15], v[32:35], v[224:227], v[0:15]
    s_waitcnt vmcnt(6) lgkmcnt(4)
    v_mfma_f32_32x32x16_bf16 v[16:31], v[44:47], v[228:231], v[16:31]
    ds_read_b64_tr_b16 v[32:33], v62 offset:12288
    ds_read_b64_tr_b16 v[34:35], v62 offset:13312
    ds_read_b64_tr_b16 v[36:37], v63 offset:12288
    ds_read_b64_tr_b16 v[38:39], v63 offset:13312
    v_mfma_f32_32x32x16_bf16 v[0:15], v[40:43], v[228:231], v[0:15]
    s_waitcnt vmcnt(5) lgkmcnt(4)
    v_mfma_f32_32x32x16_bf16 v[16:31], v[52:55], v[232:235], v[16:31]
    ds_read_b64_tr_b16 v[40:41], v60 offset:0
    ds_read_b64_tr_b16 v[42:43], v60 offset:1024
    ds_read_b64_tr_b16 v[44:45], v61 offset:0
    ds_read_b64_tr_b16 v[46:47], v61 offset:1024
    v_mfma_f32_32x32x16_bf16 v[0:15], v[48:51], v[232:235], v[0:15]
    s_waitcnt vmcnt(4) lgkmcnt(4)
    v_mfma_f32_32x32x16_bf16 v[16:31], v[36:39], v[236:239], v[16:31]
    ds_read_b64_tr_b16 v[48:49], v60 offset:4096
    ds_read_b64_tr_b16 v[50:51], v60 offset:5120
    ds_read_b64_tr_b16 v[52:53], v61 offset:4096
    ds_read_b64_tr_b16 v[54:55], v61 offset:5120
    v_mfma_f32_32x32x16_bf16 v[0:15], v[32:35], v[236:239], v[0:15]
    s_waitcnt vmcnt(3) lgkmcnt(4)
    v_mfma_f32_32x32x16_bf16 v[16:31], v[44:47], v[240:243], v[16:31]
    ds_read_b64_tr_b16 v[32:33], v60 offset:8192
    ds_read_b64_tr_b16 v[34:35], v60 offset:9216
    ds_read_b64_tr_b16 v[36:37], v61 offset:8192
    ds_read_b64_tr_b16 v[38:39], v61 offset:9216
    v_mfma_f32_32x32x16_bf16 v[0:15], v[40:43], v[240:243], v[0:15]
    s_waitcnt vmcnt(2) lgkmcnt(4)
    v_mfma_f32_32x32x16_bf16 v[16:31], v[52:55], v[244:247], v[16:31]
    ds_read_b64_tr_b16 v[40:41], v60 offset:12288
    ds_read_b64_tr_b16 v[42:43], v60 offset:13312
    ds_read_b64_tr_b16 v[44:45], v61 offset:12288
    ds_read_b64_tr_b16 v[46:47], v61 offset:13312
    v_mfma_f32_32x32x16_bf16 v[0:15], v[48:51], v[244:247], v[0:15]
    s_waitcnt vmcnt(1) lgkmcnt(4)
    v_mfma_f32_32x32x16_bf16 v[16:31], v[36:39], v[248:251], v[16:31]
    v_mfma_f32_32x32x16_bf16 v[0:15], v[32:35], v[248:251], v[0:15]
    s_waitcnt vmcnt(0) lgkmcnt(0)
    v_mfma_f32_32x32x16_bf16 v[16:31], v[44:47], v[252:255], v[16:31]
    v_mfma_f32_32x32x16_bf16 v[0:15], v[40:43], v[252:255], v[0:15]
    s_nop 7
    s_nop 2
    ds_write_b128 v78, v[16:19] offset:0
    ds_write_b128 v78, v[20:23] offset:1024
    ds_write_b128 v78, v[24:27] offset:2048
    ds_write_b128 v78, v[28:31] offset:3072
    s_waitcnt lgkmcnt(0)
    s_barrier
    ds_read_b128 v[80:83], v79 offset:0
    ds_read_b128 v[84:87], v79 offset:1024
    ds_read_b128 v[88:91], v79 offset:2048
    ds_read_b128 v[92:95], v79 offset:3072
    s_waitcnt lgkmcnt(3)
    v_pk_add_f32 v[0:1], v[0:1], v[80:81]
    v_pk_add_f32 v[2:3], v[2:3], v[82:83]
    v_cvt_pk_bf16_f32 v112, v0, v1
    v_cvt_pk_bf16_f32 v113, v2, v3
    s_waitcnt lgkmcnt(2)
    v_pk_add_f32 v[4:5], v[4:5], v[84:85]
    v_pk_add_f32 v[6:7], v[6:7], v[86:87]
    v_cvt_pk_bf16_f32 v114, v4, v5
    v_cvt_pk_bf16_f32 v115, v6, v7
    s_nop 1
    v_permlane32_swap_b32 v112, v114
    v_permlane32_swap_b32 v113, v115
    s_nop 1
    buffer_store_dwordx4 v[112:115], v76, s[24:27], s56 offen offset:0 
    s_waitcnt lgkmcnt(1)
    v_pk_add_f32 v[8:9], v[8:9], v[88:89]
    v_pk_add_f32 v[10:11], v[10:11], v[90:91]
    v_cvt_pk_bf16_f32 v116, v8, v9
    v_cvt_pk_bf16_f32 v117, v10, v11
    s_waitcnt lgkmcnt(0)
    v_pk_add_f32 v[12:13], v[12:13], v[92:93]
    v_pk_add_f32 v[14:15], v[14:15], v[94:95]
    v_cvt_pk_bf16_f32 v118, v12, v13
    v_cvt_pk_bf16_f32 v119, v14, v15
    s_nop 1
    v_permlane32_swap_b32 v116, v118
    v_permlane32_swap_b32 v117, v119
    s_nop 1
    buffer_store_dwordx4 v[116:119], v76, s[24:27], s56 offen offset:32 
    s_endpgm
    .Lmlp_end:
)ASM"
        :
        : "{s[0:1]}"(__builtin_amdgcn_kernarg_segment_ptr()), "{s2}"(__builtin_amdgcn_workgroup_id_x()), "{v0}"(__builtin_amdgcn_workitem_id_x())
        : "memory", "vcc", "scc", "s95", "v255");
}

extern "C" void kernel_launch(void* const* d_in, const int* in_sizes, int n_in, void* d_out, int out_size, void* d_ws,
                              size_t ws_size, hipStream_t stream) {
    (void)in_sizes; (void)n_in; (void)out_size; (void)ws_size;
    (void)hipFuncSetAttribute((const void*)mlp_fused, hipFuncAttributeMaxDynamicSharedMemorySize, 160 * 1024);
    mlp_fused<<<256, 512, 160 * 1024, stream>>>(d_in[0], d_in[1], d_in[2], d_in[3], d_in[4], d_out, d_ws);
}
```
